```python
import torch

def _softmax(x, axis=-1):
    return torch.softmax(x, dim=axis)
def _gelu(x):
    return torch.nn.functional.gelu(x, approximate="tanh")
def _silu(x):
    return torch.nn.functional.silu(x)
def _sigmoid(x):
    return torch.sigmoid(x)
def _layernorm(x, axis=-1, eps=1e-5):
    m = x.mean(dim=axis, keepdim=True)
    v = x.var(dim=axis, keepdim=True, unbiased=False)
    return (x - m) / torch.sqrt(v + eps)
def _scatter_add(tgt, idx, src, axis=0):
    return tgt.clone().index_add_(axis, idx, src)
def _segment_sum(src, seg, n):
    out = torch.zeros((n,) + src.shape[1:], dtype=src.dtype)
    return out.index_add_(0, seg, src)
def _pad(x, pw):
    flat = tuple(v for pair in reversed(pw) for v in pair)
    return torch.nn.functional.pad(x, flat)

kernel_name = 'gated_mix__sliding_block_cached__glu_gated_reglu__decode'

def setup_inputs():

    _w = (lambda a: (lambda t: t.to(torch.bfloat16) if t.is_floating_point() else t)(torch.as_tensor(a)))
    def _A_setup():
        B, Sq, D = 4, 1, 512
        Hq, Hkv, Sk, Dh = 32, 8, 16384, 128
        assert Hq % Hkv == 0, (Hq, Hkv)
        return {
            "x": _w((lambda *s: torch.randn(*s, dtype=torch.bfloat16) * 0.05)(B, Sq, D)),
            "k_cache": _w((lambda *s: torch.randn(*s, dtype=torch.bfloat16) * 3.2)(B, Hkv, Sk, Dh)),
            "v_cache": _w((lambda *s: torch.randn(*s, dtype=torch.bfloat16) * 0.05)(B, Hkv, Sk, Dh)),
            "wq": _w((lambda *s: torch.randn(*s, dtype=torch.bfloat16) * 3.2)(D, Hq * Dh) * (D ** -0.5)),
            "wo": _w((lambda *s: torch.randn(*s, dtype=torch.bfloat16) * 0.05)(Hq * Dh, D) * ((Hq * Dh) ** -0.5)),
        }
    _A_in = _A_setup()

    def _B_setup():
        B, S, D, F = 4, 1, 512, 1792
        return {
            "x": _w((lambda *s: torch.randn(*s, dtype=torch.bfloat16) * 0.05)(B, S, D)),
            "W_gate": _w((lambda *s: torch.randn(*s, dtype=torch.bfloat16) * 0.2)(D, F) * (2.0 / D) ** 0.5),
            "W_up": _w((lambda *s: torch.randn(*s, dtype=torch.bfloat16) * 0.2)(D, F) * (2.0 / D) ** 0.5),
            "W_down": _w((lambda *s: torch.randn(*s, dtype=torch.bfloat16) * 0.2)(F, D) * (1.0 / F) ** 0.5),
        }
    _B_in = _B_setup()

    return {
        "A_x": _A_in["x"],
        "A_k_cache": _A_in["k_cache"],
        "A_v_cache": _A_in["v_cache"],
        "A_wq": _A_in["wq"],
        "A_wo": _A_in["wo"],
        "B_x": _B_in["x"],
        "B_W_gate": _B_in["W_gate"],
        "B_W_up": _B_in["W_up"],
        "B_W_down": _B_in["W_down"],
        "gate_w": _w((lambda *s: torch.randn(*s, dtype=torch.bfloat16) * 0.05)(512) * 16.0),
    }

def reference(A_x, A_k_cache, A_v_cache, A_wq, A_wo, B_x, B_W_gate, B_W_up, B_W_down, gate_w):
    def _A_ref(x, k_cache, v_cache, wq, wo):
        B, Sq, D = x.shape
        Hq, Dh = 32, 128
        Hkv, Sk = k_cache.shape[1], k_cache.shape[2]
        W = min(4096, max(256, Sk // 4))
        rep = Hq // Hkv
        q = (lambda x, *d: x.permute(*d))((x @ wq).reshape(B, Sq, Hq, Dh), 0, 2, 1, 3)
        k = (lambda x, n, axis: torch.repeat_interleave(x, n, dim=axis))(k_cache[:, :, Sk - W:, :], rep, axis=1)
        v = (lambda x, n, axis: torch.repeat_interleave(x, n, dim=axis))(v_cache[:, :, Sk - W:, :], rep, axis=1)
        scores = torch.einsum("bhqd,bhkd->bhqk", q, k) * (Dh ** -0.5)
        p = _softmax(scores, axis=-1)
        o = torch.einsum("bhqk,bhkd->bhqd", p, v)
        return (lambda x, *d: x.permute(*d))(o, 0, 2, 1, 3).reshape(B, Sq, Hq * Dh) @ wo

    def _B_ref(x, W_gate, W_up, W_down):
        gate = torch.einsum("bsd,df->bsf", x, W_gate)
        up = torch.einsum("bsd,df->bsf", x, W_up)
        gate = gate * (lambda x, dt: x.to(dt))(gate > 0, gate.dtype)
        h = gate * up
        return torch.einsum("bsf,fd->bsd", h, W_down)

    x = A_x
    g = _sigmoid((x * gate_w).sum(-1, keepdims=True))
    a = _A_ref(x, A_k_cache, A_v_cache, A_wq, A_wo)
    b = _B_ref(x, B_W_gate, B_W_up, B_W_down)
    return g * a + (1 - g) * b
```

```cpp
#include <hip/hip_runtime.h>
#include <hip/hip_bf16.h>
#include <stdint.h>

namespace {

constexpr int B_ = 4, D_ = 512, HQ = 32, HKV = 8, SK = 16384, DH = 128, WIN = 4096, F_ = 1792;
constexpr int GQ = HQ / HKV;
constexpr int NSPLIT = 8;
constexpr int KEYS_PER_SPLIT = WIN / NSPLIT;
constexpr int NPROJ = HQ * DH + 2 * F_;
constexpr int NRB = (HQ * DH + F_) / 128;

using u32x4 = __attribute__((ext_vector_type(4))) unsigned;
using f32x4 = __attribute__((ext_vector_type(4))) float;
using f32x2 = __attribute__((ext_vector_type(2))) float;
using bf16x8 = __attribute__((ext_vector_type(8))) __bf16;

constexpr size_t WS_PROJ = 0;
constexpr size_t WS_PML = WS_PROJ + (size_t)B_ * NPROJ;
constexpr size_t WS_PO = WS_PML + (size_t)B_ * HQ * NSPLIT * 2;
constexpr size_t WS_PART = WS_PO + (size_t)B_ * HQ * NSPLIT * DH;

__device__ __forceinline__ float bf2f(unsigned short u) { return __uint_as_float(((unsigned)u) << 16); }
__device__ __forceinline__ float rbf(float x) {
  unsigned u = __float_as_uint(x);
  if ((u & 0x7fffffffu) > 0x7f800000u) return x;
  u += 0x7fffu + ((u >> 16) & 1u);
  return __uint_as_float(u & 0xffff0000u);
}
__device__ __forceinline__ unsigned pk_bf16(float lo, float hi) {
  unsigned r;
  asm volatile("v_cvt_pk_bf16_f32 %0, %1, %2" : "=v"(r) : "v"(lo), "v"(hi));
  return r;
}
__device__ __forceinline__ u32x4 ld_nt(const void* p) {
  return __builtin_nontemporal_load(reinterpret_cast<const u32x4*>(p));
}

__global__ void __launch_bounds__(512) k1_proj(const __hip_bfloat16* __restrict__ x,
                                               const __hip_bfloat16* __restrict__ wq,
                                               const __hip_bfloat16* __restrict__ wg,
                                               const __hip_bfloat16* __restrict__ wu,
                                               float* __restrict__ ws) {
  __shared__ float xs[B_][D_];
  __shared__ float red[8][B_][64];
  const int tid = threadIdx.x, lane = tid & 63, wave = tid >> 6;
  const int cb = blockIdx.x;
  const __hip_bfloat16* W;
  int ldw, c0;
  if (cb < 64) { W = wq; ldw = HQ * DH; c0 = cb * 64; }
  else if (cb < 92) { W = wg; ldw = F_; c0 = (cb - 64) * 64; }
  else { W = wu; ldw = F_; c0 = (cb - 92) * 64; }
  const int rs = lane >> 3, cc = lane & 7;
  u32x4 wv[8];
#pragma unroll
  for (int i = 0; i < 8; ++i) {
    const int row = wave * 64 + i * 8 + rs;
    wv[i] = ld_nt(W + (size_t)row * ldw + c0 + cc * 8);
  }
  for (int i = tid; i < B_ * D_; i += 512) xs[i / D_][i % D_] = __bfloat162float(x[i]);
  __syncthreads();
  float acc[B_][8];
#pragma unroll
  for (int b = 0; b < B_; ++b)
#pragma unroll
    for (int j = 0; j < 8; ++j) acc[b][j] = 0.f;
#pragma unroll
  for (int i = 0; i < 8; ++i) {
    const int row = wave * 64 + i * 8 + rs;
    float w[8];
#pragma unroll
    for (int k = 0; k < 4; ++k) {
      w[2 * k] = __uint_as_float(wv[i][k] << 16);
      w[2 * k + 1] = __uint_as_float(wv[i][k] & 0xffff0000u);
    }
#pragma unroll
    for (int b = 0; b < B_; ++b) {
      const float xv = xs[b][row];
#pragma unroll
      for (int j = 0; j < 8; ++j) acc[b][j] = fmaf(xv, w[j], acc[b][j]);
    }
  }
#pragma unroll
  for (int b = 0; b < B_; ++b)
#pragma unroll
    for (int j = 0; j < 8; ++j) {
      float v = acc[b][j];
      v += __shfl_xor(v, 8);
      v += __shfl_xor(v, 16);
      v += __shfl_xor(v, 32);
      acc[b][j] = v;
    }
  if (rs == 0) {
#pragma unroll
    for (int b = 0; b < B_; ++b)
#pragma unroll
      for (int j = 0; j < 8; ++j) red[wave][b][cc * 8 + j] = acc[b][j];
  }
  __syncthreads();
  if (tid < B_ * 64) {
    const int b = tid >> 6, col = tid & 63;
    float s = 0.f;
#pragma unroll
    for (int w = 0; w < 8; ++w) s += red[w][b][col];
    ws[WS_PROJ + (size_t)b * NPROJ + cb * 64 + col] = s;
  }
}

__global__ void __launch_bounds__(512, 2) k2_attn(const __hip_bfloat16* __restrict__ kc,
                                                  const __hip_bfloat16* __restrict__ vc,
                                                  float* __restrict__ ws) {
  __shared__ __attribute__((aligned(16))) float pex[8][64][4];
  __shared__ __attribute__((aligned(16))) float mrg[8][GQ][DH + 4];
  const int tid = threadIdx.x, lane = tid & 63, wave = tid >> 6;
  const int s = blockIdx.x, kvh = blockIdx.y, b = blockIdx.z;
  const int r = lane >> 4, c = lane & 15;
  constexpr float C2 = 0.088388347648318440f * 1.4426950408889634f;
  const size_t key0 = (size_t)(SK - WIN) + s * KEYS_PER_SPLIT + wave * 64;
  const __hip_bfloat16* kb = kc + ((size_t)(b * HKV + kvh) * SK + key0) * DH;
  const __hip_bfloat16* vb = vc + ((size_t)(b * HKV + kvh) * SK + key0) * DH;

  u32x4 qf[4];
  {
    const float* qp = ws + WS_PROJ + (size_t)b * NPROJ + (size_t)(kvh * GQ + (c & 3)) * DH + 8 * r;
#pragma unroll
    for (int j = 0; j < 4; ++j) {
      const f32x4 a0 = *reinterpret_cast<const f32x4*>(qp + 32 * j);
      const f32x4 a1 = *reinterpret_cast<const f32x4*>(qp + 32 * j + 4);
      qf[j] = u32x4{pk_bf16(a0[0], a0[1]), pk_bf16(a0[2], a0[3]), pk_bf16(a1[0], a1[1]), pk_bf16(a1[2], a1[3])};
    }
  }
  u32x4 kf[4][4];
#pragma unroll
  for (int t = 0; t < 4; ++t)
#pragma unroll
    for (int j = 0; j < 4; ++j) kf[t][j] = ld_nt(kb + (size_t)(16 * t + c) * DH + 32 * j + 8 * r);
  u32x4 vf[16];
#pragma unroll
  for (int i = 0; i < 16; ++i) vf[i] = ld_nt(vb + (size_t)(4 * i + r) * DH + 8 * c);

  f32x4 sacc = {0.f, 0.f, 0.f, 0.f};
#pragma unroll
  for (int t = 0; t < 4; ++t) {
    const bool mine = (c >> 2) == t;
#pragma unroll
    for (int j = 0; j < 4; ++j) {
      const u32x4 qz = mine ? qf[j] : u32x4{0u, 0u, 0u, 0u};
      sacc = __builtin_amdgcn_mfma_f32_16x16x32_bf16(__builtin_bit_cast(bf16x8, kf[t][j]),
                                                     __builtin_bit_cast(bf16x8, qz), sacc, 0, 0, 0);
    }
  }
  float sc[4];
#pragma unroll
  for (int i = 0; i < 4; ++i) sc[i] = sacc[i] * C2;
  float m = fmaxf(fmaxf(sc[0], sc[1]), fmaxf(sc[2], sc[3]));
  m = fmaxf(m, __shfl_xor(m, 4));
  m = fmaxf(m, __shfl_xor(m, 8));
  m = fmaxf(m, __shfl_xor(m, 16));
  m = fmaxf(m, __shfl_xor(m, 32));
  float l = 0.f;
#pragma unroll
  for (int i = 0; i < 4; ++i) {
    const float p = __builtin_amdgcn_exp2f(sc[i] - m);
    l += p;
    pex[wave][16 * (c >> 2) + 4 * r + i][c & 3] = p;
  }
  l += __shfl_xor(l, 4);
  l += __shfl_xor(l, 8);
  l += __shfl_xor(l, 16);
  l += __shfl_xor(l, 32);
  __builtin_amdgcn_s_waitcnt(0xc07f);
  __builtin_amdgcn_wave_barrier();
  f32x2 acc[GQ][4];
#pragma unroll
  for (int g = 0; g < GQ; ++g)
#pragma unroll
    for (int k = 0; k < 4; ++k) acc[g][k] = f32x2{0.f, 0.f};
#pragma unroll
  for (int i = 0; i < 16; ++i) {
    const f32x4 pr = *reinterpret_cast<const f32x4*>(&pex[wave][4 * i + r][0]);
    f32x2 v2[4];
#pragma unroll
    for (int k = 0; k < 4; ++k)
      v2[k] = f32x2{__uint_as_float(vf[i][k] << 16), __uint_as_float(vf[i][k] & 0xffff0000u)};
#pragma unroll
    for (int g = 0; g < GQ; ++g) {
      const f32x2 pg = f32x2{pr[g], pr[g]};
#pragma unroll
      for (int k = 0; k < 4; ++k) acc[g][k] = __builtin_elementwise_fma(pg, v2[k], acc[g][k]);
    }
  }
#pragma unroll
  for (int g = 0; g < GQ; ++g)
#pragma unroll
    for (int k = 0; k < 4; ++k) {
      float a0 = acc[g][k][0], a1 = acc[g][k][1];
      a0 += __shfl_xor(a0, 16); a1 += __shfl_xor(a1, 16);
      a0 += __shfl_xor(a0, 32); a1 += __shfl_xor(a1, 32);
      acc[g][k] = f32x2{a0, a1};
    }
  if (r == 0) {
#pragma unroll
    for (int g = 0; g < GQ; ++g) {
#pragma unroll
      for (int k = 0; k < 4; k += 2)
        *reinterpret_cast<f32x4*>(&mrg[wave][g][4 + 8 * c + 2 * k]) =
            f32x4{acc[g][k][0], acc[g][k][1], acc[g][k + 1][0], acc[g][k + 1][1]};
    }
    if (c < GQ) { mrg[wave][c][0] = m; mrg[wave][c][1] = l; }
  }
  __syncthreads();
  {
    const int g = tid >> 7, d = tid & 127;
    float M = -INFINITY;
#pragma unroll
    for (int w = 0; w < 8; ++w) M = fmaxf(M, mrg[w][g][0]);
    float L = 0.f, o = 0.f;
#pragma unroll
    for (int w = 0; w < 8; ++w) {
      const float wt = __builtin_amdgcn_exp2f(mrg[w][g][0] - M);
      L = fmaf(wt, mrg[w][g][1], L);
      o = fmaf(wt, mrg[w][g][4 + d], o);
    }
    const size_t slot = ((size_t)b * HQ + kvh * GQ + g) * NSPLIT + s;
    ws[WS_PO + slot * DH + d] = o;
    if (d == 0) { ws[WS_PML + 2 * slot] = M; ws[WS_PML + 2 * slot + 1] = L; }
  }
}

__global__ void __launch_bounds__(256) k3_out(const __hip_bfloat16* __restrict__ wo,
                                              const __hip_bfloat16* __restrict__ wd,
                                              float* __restrict__ ws) {
  __shared__ float act[B_][128];
  __shared__ float red[4][B_][64];
  const int tid = threadIdx.x, lane = tid & 63, wave = tid >> 6;
  const int cb = blockIdx.x, rb = blockIdx.y;
  const int rs = lane >> 3, cc = lane & 7;
  const __hip_bfloat16* W = (rb < HQ) ? (wo + (size_t)rb * 128 * D_) : (wd + (size_t)(rb - HQ) * 128 * D_);
  u32x4 wv[4];
#pragma unroll
  for (int i = 0; i < 4; ++i) {
    const int row = wave * 32 + i * 8 + rs;
    wv[i] = ld_nt(W + (size_t)row * D_ + cb * 64 + cc * 8);
  }
  {
    const int b = tid >> 6, d0 = (tid & 63) * 2;
    if (rb < HQ) {
      const size_t slot0 = ((size_t)b * HQ + rb) * NSPLIT;
      const float* pml = ws + WS_PML + 2 * slot0;
      const float* po = ws + WS_PO + slot0 * DH;
      float M = -INFINITY;
#pragma unroll
      for (int sp = 0; sp < NSPLIT; ++sp) M = fmaxf(M, pml[2 * sp]);
      float L = 0.f, o0 = 0.f, o1 = 0.f;
#pragma unroll
      for (int sp = 0; sp < NSPLIT; ++sp) {
        const float wt = __builtin_amdgcn_exp2f(pml[2 * sp] - M);
        const f32x2 a = *reinterpret_cast<const f32x2*>(po + (size_t)sp * DH + d0);
        L = fmaf(wt, pml[2 * sp + 1], L);
        o0 = fmaf(wt, a[0], o0);
        o1 = fmaf(wt, a[1], o1);
      }
      const float inv = 1.f / L;
      act[b][d0] = rbf(o0 * inv);
      act[b][d0 + 1] = rbf(o1 * inv);
    } else {
      const int f0 = (rb - HQ) * 128 + d0;
      const float* pg = ws + WS_PROJ + (size_t)b * NPROJ + HQ * DH + f0;
      const float* pu = pg + F_;
#pragma unroll
      for (int e = 0; e < 2; ++e) {
        const float gv = rbf(pg[e]), uv = rbf(pu[e]);
        const float rg = gv > 0.f ? gv : 0.f;
        act[b][d0 + e] = rbf(rg * uv);
      }
    }
  }
  __syncthreads();
  float acc[B_][8];
#pragma unroll
  for (int b = 0; b < B_; ++b)
#pragma unroll
    for (int j = 0; j < 8; ++j) acc[b][j] = 0.f;
#pragma unroll
  for (int i = 0; i < 4; ++i) {
    const int row = wave * 32 + i * 8 + rs;
    float w[8];
#pragma unroll
    for (int k = 0; k < 4; ++k) {
      w[2 * k] = __uint_as_float(wv[i][k] << 16);
      w[2 * k + 1] = __uint_as_float(wv[i][k] & 0xffff0000u);
    }
#pragma unroll
    for (int b = 0; b < B_; ++b) {
      const float av = act[b][row];
#pragma unroll
      for (int j = 0; j < 8; ++j) acc[b][j] = fmaf(av, w[j], acc[b][j]);
    }
  }
#pragma unroll
  for (int b = 0; b < B_; ++b)
#pragma unroll
    for (int j = 0; j < 8; ++j) {
      float v = acc[b][j];
      v += __shfl_xor(v, 8);
      v += __shfl_xor(v, 16);
      v += __shfl_xor(v, 32);
      acc[b][j] = v;
    }
  if (rs == 0) {
#pragma unroll
    for (int b = 0; b < B_; ++b)
#pragma unroll
      for (int j = 0; j < 8; ++j) red[wave][b][cc * 8 + j] = acc[b][j];
  }
  __syncthreads();
  {
    const int b = tid >> 6, col = tid & 63;
    const float sum = (red[0][b][col] + red[1][b][col]) + (red[2][b][col] + red[3][b][col]);
    ws[WS_PART + ((size_t)rb * B_ + b) * D_ + cb * 64 + col] = sum;
  }
}

__global__ void __launch_bounds__(64) k4_mix(const __hip_bfloat16* __restrict__ x,
                                             const __hip_bfloat16* __restrict__ gw,
                                             const float* __restrict__ ws,
                                             __hip_bfloat16* __restrict__ out) {
  const int lane = threadIdx.x;
  const int b = blockIdx.x >> 3, n = (blockIdx.x & 7) * 64 + lane;
  float d = 0.f;
#pragma unroll
  for (int i = 0; i < 8; ++i) {
    const int k = i * 64 + lane;
    d += rbf(__bfloat162float(x[b * D_ + k]) * __bfloat162float(gw[k]));
  }
#pragma unroll
  for (int o = 1; o < 64; o <<= 1) d += __shfl_xor(d, o);
  const float g = rbf(1.f / (1.f + __expf(-rbf(d))));
  float a = 0.f, f = 0.f;
#pragma unroll 8
  for (int rb = 0; rb < HQ; ++rb) a += ws[WS_PART + ((size_t)rb * B_ + b) * D_ + n];
#pragma unroll 7
  for (int rb = HQ; rb < NRB; ++rb) f += ws[WS_PART + ((size_t)rb * B_ + b) * D_ + n];
  a = rbf(a);
  f = rbf(f);
  const float res = rbf(g * a) + rbf(rbf(1.f - g) * f);
  out[b * D_ + n] = __float2bfloat16(res);
}

}

extern "C" void kernel_launch(void* const* d_in, const int* in_sizes, int n_in,
                              void* d_out, int out_size, void* d_ws, size_t ws_size,
                              hipStream_t stream) {
  const __hip_bfloat16* x = (const __hip_bfloat16*)d_in[0];
  const __hip_bfloat16* kc = (const __hip_bfloat16*)d_in[1];
  const __hip_bfloat16* vc = (const __hip_bfloat16*)d_in[2];
  const __hip_bfloat16* wq = (const __hip_bfloat16*)d_in[3];
  const __hip_bfloat16* wo = (const __hip_bfloat16*)d_in[4];
  const __hip_bfloat16* wg = (const __hip_bfloat16*)d_in[6];
  const __hip_bfloat16* wu = (const __hip_bfloat16*)d_in[7];
  const __hip_bfloat16* wd = (const __hip_bfloat16*)d_in[8];
  const __hip_bfloat16* gw = (const __hip_bfloat16*)d_in[9];
  float* ws = (float*)d_ws;
  k1_proj<<<dim3(NPROJ / 64), dim3(512), 0, stream>>>(x, wq, wg, wu, ws);
  k2_attn<<<dim3(NSPLIT, HKV, B_), dim3(512), 0, stream>>>(kc, vc, ws);
  k3_out<<<dim3(8, NRB), dim3(256), 0, stream>>>(wo, wd, ws);
  k4_mix<<<dim3(B_ * 8), dim3(64), 0, stream>>>(x, gw, ws, (__hip_bfloat16*)d_out);
}
```

```cpp
#include <hip/hip_runtime.h>
#include <hip/hip_bf16.h>
#include <stdint.h>

namespace {

constexpr int B_ = 4, D_ = 512, HQ = 32, HKV = 8, SK = 16384, DH = 128, WIN = 4096, F_ = 1792;
constexpr int GQ = HQ / HKV;
constexpr int NSPLIT = 8;
constexpr int KEYS_PER_SPLIT = WIN / NSPLIT;
constexpr int NPROJ = HQ * DH + 2 * F_;
constexpr int NRB = (HQ * DH + F_) / 128;

using u32x4 = __attribute__((ext_vector_type(4))) unsigned;
using f32x4 = __attribute__((ext_vector_type(4))) float;
using f32x2 = __attribute__((ext_vector_type(2))) float;
using bf16x8 = __attribute__((ext_vector_type(8))) __bf16;

constexpr size_t WS_PROJ = 0;
constexpr size_t WS_PML = WS_PROJ + (size_t)B_ * NPROJ;
constexpr size_t WS_PO = WS_PML + (size_t)B_ * HQ * NSPLIT * 2;
constexpr size_t WS_PART = WS_PO + (size_t)B_ * HQ * NSPLIT * DH;

__device__ __forceinline__ float bf2f(unsigned short u) { return __uint_as_float(((unsigned)u) << 16); }
__device__ __forceinline__ float rbf(float x) {
  unsigned u = __float_as_uint(x);
  if ((u & 0x7fffffffu) > 0x7f800000u) return x;
  u += 0x7fffu + ((u >> 16) & 1u);
  return __uint_as_float(u & 0xffff0000u);
}
__device__ __forceinline__ unsigned pk_bf16(float lo, float hi) {
  unsigned r;
  asm("v_cvt_pk_bf16_f32 %0, %1, %2" : "=v"(r) : "v"(lo), "v"(hi));
  return r;
}
__device__ __forceinline__ u32x4 ld_nt(const void* p) {
  return __builtin_nontemporal_load(reinterpret_cast<const u32x4*>(p));
}

__global__ void __launch_bounds__(512) k1_proj(const __hip_bfloat16* __restrict__ x,
                                               const __hip_bfloat16* __restrict__ wq,
                                               const __hip_bfloat16* __restrict__ wg,
                                               const __hip_bfloat16* __restrict__ wu,
                                               float* __restrict__ ws) {
  __shared__ float xs[B_][D_];
  __shared__ float red[8][B_][64];
  const int tid = threadIdx.x, lane = tid & 63, wave = tid >> 6;
  const int cb = blockIdx.x;
  const __hip_bfloat16* W;
  int ldw, c0;
  if (cb < 64) { W = wq; ldw = HQ * DH; c0 = cb * 64; }
  else if (cb < 92) { W = wg; ldw = F_; c0 = (cb - 64) * 64; }
  else { W = wu; ldw = F_; c0 = (cb - 92) * 64; }
  const int rs = lane >> 3, cc = lane & 7;
  u32x4 wv[8];
#pragma unroll
  for (int i = 0; i < 8; ++i) {
    const int row = wave * 64 + i * 8 + rs;
    wv[i] = ld_nt(W + (size_t)row * ldw + c0 + cc * 8);
  }
  if (tid < 256) {
    const u32x4 xv4 = *reinterpret_cast<const u32x4*>(x + tid * 8);
    float* dst = &xs[0][0] + tid * 8;
#pragma unroll
    for (int k = 0; k < 4; ++k) {
      dst[2 * k] = __uint_as_float(xv4[k] << 16);
      dst[2 * k + 1] = __uint_as_float(xv4[k] & 0xffff0000u);
    }
  }
  __syncthreads();
  float acc[B_][8];
#pragma unroll
  for (int b = 0; b < B_; ++b)
#pragma unroll
    for (int j = 0; j < 8; ++j) acc[b][j] = 0.f;
#pragma unroll
  for (int i = 0; i < 8; ++i) {
    const int row = wave * 64 + i * 8 + rs;
    float w[8];
#pragma unroll
    for (int k = 0; k < 4; ++k) {
      w[2 * k] = __uint_as_float(wv[i][k] << 16);
      w[2 * k + 1] = __uint_as_float(wv[i][k] & 0xffff0000u);
    }
#pragma unroll
    for (int b = 0; b < B_; ++b) {
      const float xv = xs[b][row];
#pragma unroll
      for (int j = 0; j < 8; ++j) acc[b][j] = fmaf(xv, w[j], acc[b][j]);
    }
  }
#pragma unroll
  for (int b = 0; b < B_; ++b)
#pragma unroll
    for (int j = 0; j < 8; ++j) {
      float v = acc[b][j];
      v += __shfl_xor(v, 8);
      v += __shfl_xor(v, 16);
      v += __shfl_xor(v, 32);
      acc[b][j] = v;
    }
  if (rs == 0) {
#pragma unroll
    for (int b = 0; b < B_; ++b)
#pragma unroll
      for (int j = 0; j < 8; ++j) red[wave][b][cc * 8 + j] = acc[b][j];
  }
  __syncthreads();
  if (tid < B_ * 64) {
    const int b = tid >> 6, col = tid & 63;
    float s = 0.f;
#pragma unroll
    for (int w = 0; w < 8; ++w) s += red[w][b][col];
    ws[WS_PROJ + (size_t)b * NPROJ + cb * 64 + col] = s;
  }
}

__global__ void __launch_bounds__(512, 2) k2_attn(const __hip_bfloat16* __restrict__ kc,
                                                  const __hip_bfloat16* __restrict__ vc,
                                                  float* __restrict__ ws) {
  __shared__ __attribute__((aligned(16))) float pex[8][64][4];
  __shared__ __attribute__((aligned(16))) float mrg[8][GQ][DH + 4];
  const int tid = threadIdx.x, lane = tid & 63, wave = tid >> 6;
  const int s = blockIdx.x, kvh = blockIdx.y, b = blockIdx.z;
  const int r = lane >> 4, c = lane & 15;
  constexpr float C2 = 0.088388347648318440f * 1.4426950408889634f;
  const size_t key0 = (size_t)(SK - WIN) + s * KEYS_PER_SPLIT + wave * 16;
  const __hip_bfloat16* kb = kc + ((size_t)(b * HKV + kvh) * SK + key0) * DH;
  const __hip_bfloat16* vb = vc + ((size_t)(b * HKV + kvh) * SK + key0) * DH;

  f32x4 qraw[8];
  {
    const float* qp = ws + WS_PROJ + (size_t)b * NPROJ + (size_t)(kvh * GQ + (c & 3)) * DH + 8 * r;
#pragma unroll
    for (int j = 0; j < 4; ++j) {
      qraw[2 * j] = *reinterpret_cast<const f32x4*>(qp + 32 * j);
      qraw[2 * j + 1] = *reinterpret_cast<const f32x4*>(qp + 32 * j + 4);
    }
  }
  u32x4 kf[4][4];
#pragma unroll
  for (int t = 0; t < 4; ++t)
#pragma unroll
    for (int j = 0; j < 4; ++j) kf[t][j] = ld_nt(kb + (size_t)(128 * t + c) * DH + 32 * j + 8 * r);
  u32x4 vf[16];
#pragma unroll
  for (int i = 0; i < 16; ++i) vf[i] = ld_nt(vb + (size_t)(128 * (i >> 2) + 4 * (i & 3) + r) * DH + 8 * c);
  __builtin_amdgcn_sched_barrier(0);

  u32x4 qf[4];
#pragma unroll
  for (int j = 0; j < 4; ++j) {
    const f32x4 a0 = qraw[2 * j], a1 = qraw[2 * j + 1];
    qf[j] = u32x4{pk_bf16(a0[0], a0[1]), pk_bf16(a0[2], a0[3]), pk_bf16(a1[0], a1[1]), pk_bf16(a1[2], a1[3])};
  }
  f32x4 sacc = {0.f, 0.f, 0.f, 0.f};
#pragma unroll
  for (int t = 0; t < 4; ++t) {
    const bool mine = (c >> 2) == t;
#pragma unroll
    for (int j = 0; j < 4; ++j) {
      const u32x4 qz = mine ? qf[j] : u32x4{0u, 0u, 0u, 0u};
      sacc = __builtin_amdgcn_mfma_f32_16x16x32_bf16(__builtin_bit_cast(bf16x8, kf[t][j]),
                                                     __builtin_bit_cast(bf16x8, qz), sacc, 0, 0, 0);
    }
  }
  float sc[4];
#pragma unroll
  for (int i = 0; i < 4; ++i) sc[i] = sacc[i] * C2;
  float m = fmaxf(fmaxf(sc[0], sc[1]), fmaxf(sc[2], sc[3]));
  m = fmaxf(m, __shfl_xor(m, 4));
  m = fmaxf(m, __shfl_xor(m, 8));
  m = fmaxf(m, __shfl_xor(m, 16));
  m = fmaxf(m, __shfl_xor(m, 32));
  float l = 0.f;
#pragma unroll
  for (int i = 0; i < 4; ++i) {
    const float p = __builtin_amdgcn_exp2f(sc[i] - m);
    l += p;
    pex[wave][16 * (c >> 2) + 4 * r + i][c & 3] = p;
  }
  l += __shfl_xor(l, 4);
  l += __shfl_xor(l, 8);
  l += __shfl_xor(l, 16);
  l += __shfl_xor(l, 32);
  __builtin_amdgcn_s_waitcnt(0xc07f);
  __builtin_amdgcn_wave_barrier();
  f32x2 acc[GQ][4];
#pragma unroll
  for (int g = 0; g < GQ; ++g)
#pragma unroll
    for (int k = 0; k < 4; ++k) acc[g][k] = f32x2{0.f, 0.f};
#pragma unroll
  for (int i = 0; i < 16; ++i) {
    const f32x4 pr = *reinterpret_cast<const f32x4*>(&pex[wave][4 * i + r][0]);
    f32x2 v2[4];
#pragma unroll
    for (int k = 0; k < 4; ++k)
      v2[k] = f32x2{__uint_as_float(vf[i][k] << 16), __uint_as_float(vf[i][k] & 0xffff0000u)};
#pragma unroll
    for (int g = 0; g < GQ; ++g) {
      const f32x2 pg = f32x2{pr[g], pr[g]};
#pragma unroll
      for (int k = 0; k < 4; ++k) acc[g][k] = __builtin_elementwise_fma(pg, v2[k], acc[g][k]);
    }
  }
#pragma unroll
  for (int g = 0; g < GQ; ++g)
#pragma unroll
    for (int k = 0; k < 4; ++k) {
      float a0 = acc[g][k][0], a1 = acc[g][k][1];
      a0 += __shfl_xor(a0, 16); a1 += __shfl_xor(a1, 16);
      a0 += __shfl_xor(a0, 32); a1 += __shfl_xor(a1, 32);
      acc[g][k] = f32x2{a0, a1};
    }
  if (r == 0) {
#pragma unroll
    for (int g = 0; g < GQ; ++g) {
#pragma unroll
      for (int k = 0; k < 4; k += 2)
        *reinterpret_cast<f32x4*>(&mrg[wave][g][4 + 8 * c + 2 * k]) =
            f32x4{acc[g][k][0], acc[g][k][1], acc[g][k + 1][0], acc[g][k + 1][1]};
    }
    if (c < GQ) { mrg[wave][c][0] = m; mrg[wave][c][1] = l; }
  }
  __syncthreads();
  {
    const int g = tid >> 7, d = tid & 127;
    float M = -INFINITY;
#pragma unroll
    for (int w = 0; w < 8; ++w) M = fmaxf(M, mrg[w][g][0]);
    float L = 0.f, o = 0.f;
#pragma unroll
    for (int w = 0; w < 8; ++w) {
      const float wt = __builtin_amdgcn_exp2f(mrg[w][g][0] - M);
      L = fmaf(wt, mrg[w][g][1], L);
      o = fmaf(wt, mrg[w][g][4 + d], o);
    }
    const size_t slot = ((size_t)b * HQ + kvh * GQ + g) * NSPLIT + s;
    ws[WS_PO + slot * DH + d] = o;
    if (d == 0) { ws[WS_PML + 2 * slot] = M; ws[WS_PML + 2 * slot + 1] = L; }
  }
}

__global__ void __launch_bounds__(256) k3_out(const __hip_bfloat16* __restrict__ wo,
                                              const __hip_bfloat16* __restrict__ wd,
                                              float* __restrict__ ws) {
  __shared__ float act[B_][128];
  __shared__ float red[4][B_][64];
  const int tid = threadIdx.x, lane = tid & 63, wave = tid >> 6;
  const int cb = blockIdx.x, rb = blockIdx.y;
  const int rs = lane >> 3, cc = lane & 7;
  const __hip_bfloat16* W = (rb < HQ) ? (wo + (size_t)rb * 128 * D_) : (wd + (size_t)(rb - HQ) * 128 * D_);
  u32x4 wv[4];
#pragma unroll
  for (int i = 0; i < 4; ++i) {
    const int row = wave * 32 + i * 8 + rs;
    wv[i] = ld_nt(W + (size_t)row * D_ + cb * 64 + cc * 8);
  }
  {
    const int b = tid >> 6, d0 = (tid & 63) * 2;
    if (rb < HQ) {
      const size_t slot0 = ((size_t)b * HQ + rb) * NSPLIT;
      const float* pml = ws + WS_PML + 2 * slot0;
      const float* po = ws + WS_PO + slot0 * DH;
      f32x2 ml[NSPLIT], av[NSPLIT];
#pragma unroll
      for (int sp = 0; sp < NSPLIT; ++sp) {
        ml[sp] = *reinterpret_cast<const f32x2*>(pml + 2 * sp);
        av[sp] = *reinterpret_cast<const f32x2*>(po + (size_t)sp * DH + d0);
      }
      float M = -INFINITY;
#pragma unroll
      for (int sp = 0; sp < NSPLIT; ++sp) M = fmaxf(M, ml[sp][0]);
      float L = 0.f, o0 = 0.f, o1 = 0.f;
#pragma unroll
      for (int sp = 0; sp < NSPLIT; ++sp) {
        const float wt = __builtin_amdgcn_exp2f(ml[sp][0] - M);
        L = fmaf(wt, ml[sp][1], L);
        o0 = fmaf(wt, av[sp][0], o0);
        o1 = fmaf(wt, av[sp][1], o1);
      }
      const float inv = 1.f / L;
      act[b][d0] = rbf(o0 * inv);
      act[b][d0 + 1] = rbf(o1 * inv);
    } else {
      const int f0 = (rb - HQ) * 128 + d0;
      const float* pg = ws + WS_PROJ + (size_t)b * NPROJ + HQ * DH + f0;
      const float* pu = pg + F_;
#pragma unroll
      for (int e = 0; e < 2; ++e) {
        const float gv = rbf(pg[e]), uv = rbf(pu[e]);
        const float rg = gv > 0.f ? gv : 0.f;
        act[b][d0 + e] = rbf(rg * uv);
      }
    }
  }
  __syncthreads();
  float acc[B_][8];
#pragma unroll
  for (int b = 0; b < B_; ++b)
#pragma unroll
    for (int j = 0; j < 8; ++j) acc[b][j] = 0.f;
#pragma unroll
  for (int i = 0; i < 4; ++i) {
    const int row = wave * 32 + i * 8 + rs;
    float w[8];
#pragma unroll
    for (int k = 0; k < 4; ++k) {
      w[2 * k] = __uint_as_float(wv[i][k] << 16);
      w[2 * k + 1] = __uint_as_float(wv[i][k] & 0xffff0000u);
    }
#pragma unroll
    for (int b = 0; b < B_; ++b) {
      const float av = act[b][row];
#pragma unroll
      for (int j = 0; j < 8; ++j) acc[b][j] = fmaf(av, w[j], acc[b][j]);
    }
  }
#pragma unroll
  for (int b = 0; b < B_; ++b)
#pragma unroll
    for (int j = 0; j < 8; ++j) {
      float v = acc[b][j];
      v += __shfl_xor(v, 8);
      v += __shfl_xor(v, 16);
      v += __shfl_xor(v, 32);
      acc[b][j] = v;
    }
  if (rs == 0) {
#pragma unroll
    for (int b = 0; b < B_; ++b)
#pragma unroll
      for (int j = 0; j < 8; ++j) red[wave][b][cc * 8 + j] = acc[b][j];
  }
  __syncthreads();
  {
    const int b = tid >> 6, col = tid & 63;
    const float sum = (red[0][b][col] + red[1][b][col]) + (red[2][b][col] + red[3][b][col]);
    ws[WS_PART + ((size_t)rb * B_ + b) * D_ + cb * 64 + col] = sum;
  }
}

__global__ void __launch_bounds__(64) k4_mix(const __hip_bfloat16* __restrict__ x,
                                             const __hip_bfloat16* __restrict__ gw,
                                             const float* __restrict__ ws,
                                             __hip_bfloat16* __restrict__ out) {
  const int lane = threadIdx.x;
  const int b = blockIdx.x >> 3, n = (blockIdx.x & 7) * 64 + lane;
  float pv[NRB];
#pragma unroll
  for (int rb = 0; rb < NRB; ++rb) pv[rb] = ws[WS_PART + ((size_t)rb * B_ + b) * D_ + n];
  const u32x4 xr = *reinterpret_cast<const u32x4*>(x + b * D_ + lane * 8);
  const u32x4 gr = *reinterpret_cast<const u32x4*>(gw + lane * 8);
  float d = 0.f;
#pragma unroll
  for (int k = 0; k < 4; ++k) {
    d += rbf(__uint_as_float(xr[k] << 16) * __uint_as_float(gr[k] << 16));
    d += rbf(__uint_as_float(xr[k] & 0xffff0000u) * __uint_as_float(gr[k] & 0xffff0000u));
  }
#pragma unroll
  for (int o = 1; o < 64; o <<= 1) d += __shfl_xor(d, o);
  const float g = rbf(1.f / (1.f + __expf(-rbf(d))));
  float a = 0.f, f = 0.f;
#pragma unroll
  for (int rb = 0; rb < HQ; ++rb) a += pv[rb];
#pragma unroll
  for (int rb = HQ; rb < NRB; ++rb) f += pv[rb];
  a = rbf(a);
  f = rbf(f);
  const float res = rbf(g * a) + rbf(rbf(1.f - g) * f);
  out[b * D_ + n] = __float2bfloat16(res);
}

}

extern "C" void kernel_launch(void* const* d_in, const int* in_sizes, int n_in,
                              void* d_out, int out_size, void* d_ws, size_t ws_size,
                              hipStream_t stream) {
  const __hip_bfloat16* x = (const __hip_bfloat16*)d_in[0];
  const __hip_bfloat16* kc = (const __hip_bfloat16*)d_in[1];
  const __hip_bfloat16* vc = (const __hip_bfloat16*)d_in[2];
  const __hip_bfloat16* wq = (const __hip_bfloat16*)d_in[3];
  const __hip_bfloat16* wo = (const __hip_bfloat16*)d_in[4];
  const __hip_bfloat16* wg = (const __hip_bfloat16*)d_in[6];
  const __hip_bfloat16* wu = (const __hip_bfloat16*)d_in[7];
  const __hip_bfloat16* wd = (const __hip_bfloat16*)d_in[8];
  const __hip_bfloat16* gw = (const __hip_bfloat16*)d_in[9];
  float* ws = (float*)d_ws;
  k1_proj<<<dim3(NPROJ / 64), dim3(512), 0, stream>>>(x, wq, wg, wu, ws);
  k2_attn<<<dim3(NSPLIT, HKV, B_), dim3(512), 0, stream>>>(kc, vc, ws);
  k3_out<<<dim3(8, NRB), dim3(256), 0, stream>>>(wo, wd, ws);
  k4_mix<<<dim3(B_ * 8), dim3(64), 0, stream>>>(x, gw, ws, (__hip_bfloat16*)d_out);
}
```

```cpp
#include <hip/hip_runtime.h>
#include <hip/hip_bf16.h>
#include <stdint.h>

namespace {

constexpr int B_ = 4, D_ = 512, HQ = 32, HKV = 8, SK = 16384, DH = 128, WIN = 4096, F_ = 1792;
constexpr int GQ = HQ / HKV;
constexpr int NSPLIT = 8;
constexpr int KEYS_PER_SPLIT = WIN / NSPLIT;
constexpr int NPROJ = HQ * DH + 2 * F_;
constexpr int FROWS = F_ / HKV;

using u32x4 = __attribute__((ext_vector_type(4))) unsigned;
using f32x4 = __attribute__((ext_vector_type(4))) float;
using f32x2 = __attribute__((ext_vector_type(2))) float;
using bf16x8 = __attribute__((ext_vector_type(8))) __bf16;
typedef __attribute__((address_space(1))) unsigned gu32;
typedef __attribute__((address_space(1))) unsigned long long gu64;

constexpr size_t WS_CNT = 0;
constexpr size_t WS_PROJ = 256;
constexpr size_t WS_PML = WS_PROJ + (size_t)2 * B_ * NPROJ;
constexpr size_t WS_PO = WS_PML + (size_t)B_ * HQ * NSPLIT * 2;
constexpr size_t WS_Y = WS_PO + (size_t)B_ * HQ * NSPLIT * DH;

__device__ __forceinline__ float rbf(float x) {
  unsigned u = __float_as_uint(x);
  if ((u & 0x7fffffffu) > 0x7f800000u) return x;
  u += 0x7fffu + ((u >> 16) & 1u);
  return __uint_as_float(u & 0xffff0000u);
}
__device__ __forceinline__ unsigned pk_bf16(float lo, float hi) {
  unsigned r;
  asm("v_cvt_pk_bf16_f32 %0, %1, %2" : "=v"(r) : "v"(lo), "v"(hi));
  return r;
}
__device__ __forceinline__ u32x4 ld_nt(const void* p) {
  return __builtin_nontemporal_load(reinterpret_cast<const u32x4*>(p));
}
__device__ __forceinline__ void st_sc1_f2(float* p, float a, float b) {
  const unsigned long long v = ((unsigned long long)__float_as_uint(b) << 32) | __float_as_uint(a);
  __hip_atomic_store((gu64*)p, v, __ATOMIC_RELAXED, __HIP_MEMORY_SCOPE_AGENT);
}
__device__ __forceinline__ float ld_sc1_f(const float* p) {
  return __uint_as_float(__hip_atomic_load((gu32*)p, __ATOMIC_RELAXED, __HIP_MEMORY_SCOPE_AGENT));
}
__device__ __forceinline__ f32x2 ld_sc1_f2(const float* p) {
  const unsigned long long v = __hip_atomic_load((gu64*)p, __ATOMIC_RELAXED, __HIP_MEMORY_SCOPE_AGENT);
  return f32x2{__uint_as_float((unsigned)v), __uint_as_float((unsigned)(v >> 32))};
}

__global__ void __launch_bounds__(512) ka_proj(const __hip_bfloat16* __restrict__ x,
                                               const __hip_bfloat16* __restrict__ wq,
                                               const __hip_bfloat16* __restrict__ wg,
                                               const __hip_bfloat16* __restrict__ wu,
                                               float* __restrict__ ws) {
  __shared__ float xs[B_][D_];
  __shared__ float red[8][B_][64];
  const int tid = threadIdx.x, lane = tid & 63, wave = tid >> 6;
  const int cb = blockIdx.x >> 1, kh = blockIdx.x & 1;
  if (blockIdx.x == 0 && tid < 64) reinterpret_cast<unsigned*>(ws)[WS_CNT + tid] = 0u;
  const __hip_bfloat16* W;
  int ldw, c0;
  if (cb < 64) { W = wq; ldw = HQ * DH; c0 = cb * 64; }
  else if (cb < 92) { W = wg; ldw = F_; c0 = (cb - 64) * 64; }
  else { W = wu; ldw = F_; c0 = (cb - 92) * 64; }
  const int rs = lane >> 3, cc = lane & 7;
  u32x4 wv[4];
#pragma unroll
  for (int i = 0; i < 4; ++i) {
    const int row = kh * 256 + wave * 32 + i * 8 + rs;
    wv[i] = ld_nt(W + (size_t)row * ldw + c0 + cc * 8);
  }
  if (tid < 256) {
    const u32x4 xv4 = *reinterpret_cast<const u32x4*>(x + tid * 8);
    float* dst = &xs[0][0] + tid * 8;
#pragma unroll
    for (int k = 0; k < 4; ++k) {
      dst[2 * k] = __uint_as_float(xv4[k] << 16);
      dst[2 * k + 1] = __uint_as_float(xv4[k] & 0xffff0000u);
    }
  }
  __syncthreads();
  float acc[B_][8];
#pragma unroll
  for (int b = 0; b < B_; ++b)
#pragma unroll
    for (int j = 0; j < 8; ++j) acc[b][j] = 0.f;
#pragma unroll
  for (int i = 0; i < 4; ++i) {
    const int row = kh * 256 + wave * 32 + i * 8 + rs;
    float w[8];
#pragma unroll
    for (int k = 0; k < 4; ++k) {
      w[2 * k] = __uint_as_float(wv[i][k] << 16);
      w[2 * k + 1] = __uint_as_float(wv[i][k] & 0xffff0000u);
    }
#pragma unroll
    for (int b = 0; b < B_; ++b) {
      const float xv = xs[b][row];
#pragma unroll
      for (int j = 0; j < 8; ++j) acc[b][j] = fmaf(xv, w[j], acc[b][j]);
    }
  }
#pragma unroll
  for (int b = 0; b < B_; ++b)
#pragma unroll
    for (int j = 0; j < 8; ++j) {
      float v = acc[b][j];
      v += __shfl_xor(v, 8);
      v += __shfl_xor(v, 16);
      v += __shfl_xor(v, 32);
      acc[b][j] = v;
    }
  if (rs == 0) {
#pragma unroll
    for (int b = 0; b < B_; ++b)
#pragma unroll
      for (int j = 0; j < 8; ++j) red[wave][b][cc * 8 + j] = acc[b][j];
  }
  __syncthreads();
  if (tid < B_ * 64) {
    const int b = tid >> 6, col = tid & 63;
    float s = 0.f;
#pragma unroll
    for (int w = 0; w < 8; ++w) s += red[w][b][col];
    ws[WS_PROJ + ((size_t)kh * B_ + b) * NPROJ + cb * 64 + col] = s;
  }
}

__device__ __forceinline__ void wait_count(gu32* cnt, unsigned target) {
  for (unsigned spins = 0; spins < (1u << 22); ++spins) {
    if (__hip_atomic_load(cnt, __ATOMIC_RELAXED, __HIP_MEMORY_SCOPE_AGENT) == target) return;
    __builtin_amdgcn_s_sleep(1);
  }
}

__global__ void __launch_bounds__(512, 2) kb_attn(const __hip_bfloat16* __restrict__ kc,
                                                  const __hip_bfloat16* __restrict__ vc,
                                                  const __hip_bfloat16* __restrict__ wo,
                                                  const __hip_bfloat16* __restrict__ wd,
                                                  const __hip_bfloat16* __restrict__ x,
                                                  const __hip_bfloat16* __restrict__ gw,
                                                  float* __restrict__ ws,
                                                  __hip_bfloat16* __restrict__ out) {
  __shared__ __attribute__((aligned(16))) float pex[8][64][4];
  __shared__ __attribute__((aligned(16))) float mrg[8][GQ][DH + 4];
  __shared__ __attribute__((aligned(16))) unsigned qs[GQ * DH / 2];
  __shared__ __attribute__((aligned(16))) float act[GQ * DH + FROWS + 32];
  __shared__ __attribute__((aligned(16))) float red[8][2][64];
  __shared__ unsigned lastflag;
  const int tid = threadIdx.x, lane = tid & 63, wave = tid >> 6;
  const int s = blockIdx.x, kvh = blockIdx.y, b = blockIdx.z;
  const int r = lane >> 4, c = lane & 15;
  constexpr float C2 = 0.088388347648318440f * 1.4426950408889634f;
  const size_t key0 = (size_t)(SK - WIN) + s * KEYS_PER_SPLIT + wave * 16;
  const __hip_bfloat16* kb = kc + ((size_t)(b * HKV + kvh) * SK + key0) * DH;
  const __hip_bfloat16* vb = vc + ((size_t)(b * HKV + kvh) * SK + key0) * DH;

  const float* pj = ws + WS_PROJ;
  const float q0 = pj[(size_t)b * NPROJ + kvh * 512 + tid];
  const float q1 = pj[(size_t)(B_ + b) * NPROJ + kvh * 512 + tid];
  float g0 = 0.f, g1 = 0.f, u0 = 0.f, u1 = 0.f;
  if (tid < FROWS) {
    const int f = kvh * FROWS + tid;
    g0 = pj[(size_t)b * NPROJ + HQ * DH + f];
    g1 = pj[(size_t)(B_ + b) * NPROJ + HQ * DH + f];
    u0 = pj[(size_t)b * NPROJ + HQ * DH + F_ + f];
    u1 = pj[(size_t)(B_ + b) * NPROJ + HQ * DH + F_ + f];
  }
  u32x4 kf[4][4];
#pragma unroll
  for (int t = 0; t < 4; ++t)
#pragma unroll
    for (int j = 0; j < 4; ++j) kf[t][j] = ld_nt(kb + (size_t)(128 * t + c) * DH + 32 * j + 8 * r);
  u32x4 vf[16];
#pragma unroll
  for (int i = 0; i < 16; ++i) vf[i] = ld_nt(vb + (size_t)(128 * (i >> 2) + 4 * (i & 3) + r) * DH + 8 * c);
  __builtin_amdgcn_sched_barrier(0);

  {
    const float qv = q0 + q1;
    const float qn = __shfl_down(qv, 1);
    if ((tid & 1) == 0) qs[tid >> 1] = pk_bf16(qv, qn);
    if (tid < FROWS) {
      const float gv = rbf(g0 + g1), uv = rbf(u0 + u1);
      act[GQ * DH + tid] = rbf((gv > 0.f ? gv : 0.f) * uv);
    }
  }
  __syncthreads();
  u32x4 qf[4];
#pragma unroll
  for (int j = 0; j < 4; ++j) qf[j] = *reinterpret_cast<const u32x4*>(&qs[((c & 3) * DH + 32 * j + 8 * r) >> 1]);

  f32x4 sacc = {0.f, 0.f, 0.f, 0.f};
#pragma unroll
  for (int t = 0; t < 4; ++t) {
    const bool mine = (c >> 2) == t;
#pragma unroll
    for (int j = 0; j < 4; ++j) {
      const u32x4 qz = mine ? qf[j] : u32x4{0u, 0u, 0u, 0u};
      sacc = __builtin_amdgcn_mfma_f32_16x16x32_bf16(__builtin_bit_cast(bf16x8, kf[t][j]),
                                                     __builtin_bit_cast(bf16x8, qz), sacc, 0, 0, 0);
    }
  }
  __builtin_amdgcn_sched_barrier(0);
  const int rs = lane >> 3, cc = lane & 7;
  u32x4 wv[12];
#pragma unroll
  for (int i = 0; i < 8; ++i)
    wv[i] = *reinterpret_cast<const u32x4*>(wo + (size_t)(kvh * 512 + i * 64 + wave * 8 + rs) * D_ + s * 64 + cc * 8);
#pragma unroll
  for (int i = 0; i < 4; ++i) {
    const int row = i * 64 + wave * 8 + rs;
    wv[8 + i] = (row < FROWS) ? *reinterpret_cast<const u32x4*>(wd + (size_t)(kvh * FROWS + row) * D_ + s * 64 + cc * 8)
                              : u32x4{0u, 0u, 0u, 0u};
  }
  __builtin_amdgcn_sched_barrier(0);

  float sc[4];
#pragma unroll
  for (int i = 0; i < 4; ++i) sc[i] = sacc[i] * C2;
  float m = fmaxf(fmaxf(sc[0], sc[1]), fmaxf(sc[2], sc[3]));
  m = fmaxf(m, __shfl_xor(m, 4));
  m = fmaxf(m, __shfl_xor(m, 8));
  m = fmaxf(m, __shfl_xor(m, 16));
  m = fmaxf(m, __shfl_xor(m, 32));
  float l = 0.f;
#pragma unroll
  for (int i = 0; i < 4; ++i) {
    const float p = __builtin_amdgcn_exp2f(sc[i] - m);
    l += p;
    pex[wave][16 * (c >> 2) + 4 * r + i][c & 3] = p;
  }
  l += __shfl_xor(l, 4);
  l += __shfl_xor(l, 8);
  l += __shfl_xor(l, 16);
  l += __shfl_xor(l, 32);
  __builtin_amdgcn_s_waitcnt(0xc07f);
  __builtin_amdgcn_wave_barrier();
  f32x2 acc[GQ][4];
#pragma unroll
  for (int g = 0; g < GQ; ++g)
#pragma unroll
    for (int k = 0; k < 4; ++k) acc[g][k] = f32x2{0.f, 0.f};
#pragma unroll
  for (int i = 0; i < 16; ++i) {
    const f32x4 pr = *reinterpret_cast<const f32x4*>(&pex[wave][4 * i + r][0]);
    f32x2 v2[4];
#pragma unroll
    for (int k = 0; k < 4; ++k)
      v2[k] = f32x2{__uint_as_float(vf[i][k] << 16), __uint_as_float(vf[i][k] & 0xffff0000u)};
#pragma unroll
    for (int g = 0; g < GQ; ++g) {
      const f32x2 pg = f32x2{pr[g], pr[g]};
#pragma unroll
      for (int k = 0; k < 4; ++k) acc[g][k] = __builtin_elementwise_fma(pg, v2[k], acc[g][k]);
    }
  }
#pragma unroll
  for (int g = 0; g < GQ; ++g)
#pragma unroll
    for (int k = 0; k < 4; ++k) {
      float a0 = acc[g][k][0], a1 = acc[g][k][1];
      a0 += __shfl_xor(a0, 16); a1 += __shfl_xor(a1, 16);
      a0 += __shfl_xor(a0, 32); a1 += __shfl_xor(a1, 32);
      acc[g][k] = f32x2{a0, a1};
    }
  if (r == 0) {
#pragma unroll
    for (int g = 0; g < GQ; ++g) {
#pragma unroll
      for (int k = 0; k < 4; k += 2)
        *reinterpret_cast<f32x4*>(&mrg[wave][g][4 + 8 * c + 2 * k]) =
            f32x4{acc[g][k][0], acc[g][k][1], acc[g][k + 1][0], acc[g][k + 1][1]};
    }
    if (c < GQ) { mrg[wave][c][0] = m; mrg[wave][c][1] = l; }
  }
  __syncthreads();
  const int g = tid >> 7, d = tid & 127;
  {
    float M = -INFINITY;
#pragma unroll
    for (int w = 0; w < 8; ++w) M = fmaxf(M, mrg[w][g][0]);
    float L = 0.f, o = 0.f;
#pragma unroll
    for (int w = 0; w < 8; ++w) {
      const float wt = __builtin_amdgcn_exp2f(mrg[w][g][0] - M);
      L = fmaf(wt, mrg[w][g][1], L);
      o = fmaf(wt, mrg[w][g][4 + d], o);
    }
    const float on = __shfl_down(o, 1);
    const size_t slot = ((size_t)b * HQ + kvh * GQ + g) * NSPLIT + s;
    if ((d & 1) == 0) st_sc1_f2(ws + WS_PO + slot * DH + d, o, on);
    if (d == 0) st_sc1_f2(ws + WS_PML + 2 * slot, M, L);
  }
  asm volatile("s_waitcnt vmcnt(0)" ::: "memory");
  __syncthreads();
  gu32* cnt1 = (gu32*)(reinterpret_cast<unsigned*>(ws) + WS_CNT + b * HKV + kvh);
  if (tid == 0) {
    __hip_atomic_fetch_add(cnt1, 1u, __ATOMIC_RELAXED, __HIP_MEMORY_SCOPE_AGENT);
    wait_count(cnt1, NSPLIT);
  }
  __syncthreads();
  {
    const size_t slot0 = ((size_t)b * HQ + kvh * GQ + g) * NSPLIT;
    f32x2 ml[NSPLIT];
    float av[NSPLIT];
#pragma unroll
    for (int sp = 0; sp < NSPLIT; ++sp) {
      ml[sp] = ld_sc1_f2(ws + WS_PML + 2 * (slot0 + sp));
      av[sp] = ld_sc1_f(ws + WS_PO + (slot0 + sp) * DH + d);
    }
    float M = -INFINITY;
#pragma unroll
    for (int sp = 0; sp < NSPLIT; ++sp) M = fmaxf(M, ml[sp][0]);
    float L = 0.f, o = 0.f;
#pragma unroll
    for (int sp = 0; sp < NSPLIT; ++sp) {
      const float wt = __builtin_amdgcn_exp2f(ml[sp][0] - M);
      L = fmaf(wt, ml[sp][1], L);
      o = fmaf(wt, av[sp], o);
    }
    act[tid] = rbf(o / L);
  }
  __syncthreads();
  {
    float ya[8], yf[8];
#pragma unroll
    for (int j = 0; j < 8; ++j) { ya[j] = 0.f; yf[j] = 0.f; }
#pragma unroll
    for (int i = 0; i < 12; ++i) {
      const int row = (i < 8) ? (i * 64 + wave * 8 + rs) : (GQ * DH + (i - 8) * 64 + wave * 8 + rs);
      const float av = (i < 8 || (row - GQ * DH) < FROWS) ? act[row] : 0.f;
      float* y = (i < 8) ? ya : yf;
#pragma unroll
      for (int k = 0; k < 4; ++k) {
        y[2 * k] = fmaf(av, __uint_as_float(wv[i][k] << 16), y[2 * k]);
        y[2 * k + 1] = fmaf(av, __uint_as_float(wv[i][k] & 0xffff0000u), y[2 * k + 1]);
      }
    }
#pragma unroll
    for (int j = 0; j < 8; ++j) {
      float v = ya[j], w = yf[j];
      v += __shfl_xor(v, 8); w += __shfl_xor(w, 8);
      v += __shfl_xor(v, 16); w += __shfl_xor(w, 16);
      v += __shfl_xor(v, 32); w += __shfl_xor(w, 32);
      ya[j] = v; yf[j] = w;
    }
    if (rs == 0) {
#pragma unroll
      for (int j = 0; j < 8; ++j) { red[wave][0][cc * 8 + j] = ya[j]; red[wave][1][cc * 8 + j] = yf[j]; }
    }
  }
  __syncthreads();
  float* ybase = ws + WS_Y + (size_t)(b * NSPLIT + s) * HKV * 128;
  if (tid < 64) {
    const int which = tid >> 5, col = (tid & 31) * 2;
    float s0 = 0.f, s1 = 0.f;
#pragma unroll
    for (int w = 0; w < 8; ++w) { s0 += red[w][which][col]; s1 += red[w][which][col + 1]; }
    st_sc1_f2(ybase + kvh * 128 + which * 64 + col, s0, s1);
    asm volatile("s_waitcnt vmcnt(0)" ::: "memory");
    if (tid == 0) {
      gu32* cnt2 = (gu32*)(reinterpret_cast<unsigned*>(ws) + WS_CNT + 32 + b * NSPLIT + s);
      const unsigned old = __hip_atomic_fetch_add(cnt2, 1u, __ATOMIC_RELAXED, __HIP_MEMORY_SCOPE_AGENT);
      lastflag = (old == HKV - 1) ? 1u : 0u;
    }
    __builtin_amdgcn_s_waitcnt(0xc07f);
    __builtin_amdgcn_wave_barrier();
    if (lastflag) {
      const int n = s * 64 + lane;
      float a = 0.f, f = 0.f;
      float av[HKV], fv[HKV];
#pragma unroll
      for (int h = 0; h < HKV; ++h) {
        av[h] = ld_sc1_f(ybase + h * 128 + lane);
        fv[h] = ld_sc1_f(ybase + h * 128 + 64 + lane);
      }
      const u32x4 xr = *reinterpret_cast<const u32x4*>(x + b * D_ + lane * 8);
      const u32x4 gr = *reinterpret_cast<const u32x4*>(gw + lane * 8);
      float dd = 0.f;
#pragma unroll
      for (int k = 0; k < 4; ++k) {
        dd += rbf(__uint_as_float(xr[k] << 16) * __uint_as_float(gr[k] << 16));
        dd += rbf(__uint_as_float(xr[k] & 0xffff0000u) * __uint_as_float(gr[k] & 0xffff0000u));
      }
#pragma unroll
      for (int o = 1; o < 64; o <<= 1) dd += __shfl_xor(dd, o);
      const float gt = rbf(1.f / (1.f + __expf(-rbf(dd))));
#pragma unroll
      for (int h = 0; h < HKV; ++h) { a += av[h]; f += fv[h]; }
      a = rbf(a);
      f = rbf(f);
      const float res = rbf(gt * a) + rbf(rbf(1.f - gt) * f);
      out[b * D_ + n] = __float2bfloat16(res);
    }
  }
}

}

extern "C" void kernel_launch(void* const* d_in, const int* in_sizes, int n_in,
                              void* d_out, int out_size, void* d_ws, size_t ws_size,
                              hipStream_t stream) {
  const __hip_bfloat16* x = (const __hip_bfloat16*)d_in[0];
  const __hip_bfloat16* kc = (const __hip_bfloat16*)d_in[1];
  const __hip_bfloat16* vc = (const __hip_bfloat16*)d_in[2];
  const __hip_bfloat16* wq = (const __hip_bfloat16*)d_in[3];
  const __hip_bfloat16* wo = (const __hip_bfloat16*)d_in[4];
  const __hip_bfloat16* wg = (const __hip_bfloat16*)d_in[6];
  const __hip_bfloat16* wu = (const __hip_bfloat16*)d_in[7];
  const __hip_bfloat16* wd = (const __hip_bfloat16*)d_in[8];
  const __hip_bfloat16* gw = (const __hip_bfloat16*)d_in[9];
  float* ws = (float*)d_ws;
  ka_proj<<<dim3(2 * NPROJ / 64), dim3(512), 0, stream>>>(x, wq, wg, wu, ws);
  kb_attn<<<dim3(NSPLIT, HKV, B_), dim3(512), 0, stream>>>(kc, vc, wo, wd, x, gw, ws,
                                                           (__hip_bfloat16*)d_out);
}
```

```cpp
#include <hip/hip_runtime.h>
#include <hip/hip_bf16.h>
#include <stdint.h>

#define STAMPAT(i) do {} while (0)
namespace {

constexpr int B_ = 4, D_ = 512, HQ = 32, HKV = 8, SK = 16384, DH = 128, WIN = 4096, F_ = 1792;
constexpr int GQ = HQ / HKV;
constexpr int NSPLIT = 8;
constexpr int KEYS_PER_SPLIT = WIN / NSPLIT;
constexpr int NPROJ = HQ * DH + 2 * F_;
constexpr int FROWS = F_ / HKV;

using u32x4 = __attribute__((ext_vector_type(4))) unsigned;
using f32x4 = __attribute__((ext_vector_type(4))) float;
using f32x2 = __attribute__((ext_vector_type(2))) float;
using bf16x8 = __attribute__((ext_vector_type(8))) __bf16;
typedef __attribute__((address_space(1))) unsigned gu32;
typedef __attribute__((address_space(1))) unsigned long long gu64;

constexpr size_t WS_CNT = 0;
constexpr size_t WS_PROJ = 256;
constexpr size_t WS_GR = WS_PROJ + (size_t)2 * B_ * NPROJ;
constexpr int PREC = DH + 2;
constexpr size_t NGR_PO = (size_t)B_ * HQ * NSPLIT * PREC;
constexpr size_t NGR_Y = (size_t)B_ * NSPLIT * HKV * 128;
constexpr size_t NGR = NGR_PO + NGR_Y;
constexpr unsigned TAG = 0x13579bdfu;

__device__ __forceinline__ float rbf(float x) {
  unsigned u = __float_as_uint(x);
  if ((u & 0x7fffffffu) > 0x7f800000u) return x;
  u += 0x7fffu + ((u >> 16) & 1u);
  return __uint_as_float(u & 0xffff0000u);
}
__device__ __forceinline__ unsigned pk_bf16(float lo, float hi) {
  unsigned r;
  asm("v_cvt_pk_bf16_f32 %0, %1, %2" : "=v"(r) : "v"(lo), "v"(hi));
  return r;
}
__device__ __forceinline__ u32x4 ld_nt(const void* p) {
  return __builtin_nontemporal_load(reinterpret_cast<const u32x4*>(p));
}
__device__ __forceinline__ void st_gr(unsigned long long* g, float v) {
  const unsigned long long w = ((unsigned long long)TAG << 32) | __float_as_uint(v);
  __hip_atomic_store((gu64*)g, w, __ATOMIC_RELAXED, __HIP_MEMORY_SCOPE_AGENT);
}
__device__ __forceinline__ unsigned long long ld_gr(const unsigned long long* g) {
  return __hip_atomic_load((gu64*)g, __ATOMIC_RELAXED, __HIP_MEMORY_SCOPE_AGENT);
}
__device__ __forceinline__ void lds_barrier() {
  asm volatile("s_waitcnt lgkmcnt(0)\n\ts_barrier" ::: "memory");
}
__device__ __forceinline__ void st_sc1_f2(float* p, float a, float b) {
  const unsigned long long v = ((unsigned long long)__float_as_uint(b) << 32) | __float_as_uint(a);
  __hip_atomic_store((gu64*)p, v, __ATOMIC_RELAXED, __HIP_MEMORY_SCOPE_AGENT);
}
__device__ __forceinline__ float ld_sc1_f(const float* p) {
  return __uint_as_float(__hip_atomic_load((gu32*)p, __ATOMIC_RELAXED, __HIP_MEMORY_SCOPE_AGENT));
}
__device__ __forceinline__ f32x2 ld_sc1_f2(const float* p) {
  const unsigned long long v = __hip_atomic_load((gu64*)p, __ATOMIC_RELAXED, __HIP_MEMORY_SCOPE_AGENT);
  return f32x2{__uint_as_float((unsigned)v), __uint_as_float((unsigned)(v >> 32))};
}

__global__ void __launch_bounds__(512) ka_proj(const __hip_bfloat16* __restrict__ x,
                                               const __hip_bfloat16* __restrict__ wq,
                                               const __hip_bfloat16* __restrict__ wg,
                                               const __hip_bfloat16* __restrict__ wu,
                                               float* __restrict__ ws) {
  __shared__ float xs[B_][D_];
  __shared__ float red[8][B_][64];
  const int tid = threadIdx.x, lane = tid & 63, wave = tid >> 6;
  const int cb = blockIdx.x >> 1, kh = blockIdx.x & 1;
  {
    u32x4* gz = reinterpret_cast<u32x4*>(ws + WS_GR);
    for (size_t i = (size_t)blockIdx.x * 512 + tid; i < NGR / 2; i += (size_t)gridDim.x * 512) gz[i] = u32x4{0u, 0u, 0u, 0u};
  }
  const __hip_bfloat16* W;
  int ldw, c0;
  if (cb < 64) { W = wq; ldw = HQ * DH; c0 = cb * 64; }
  else if (cb < 92) { W = wg; ldw = F_; c0 = (cb - 64) * 64; }
  else { W = wu; ldw = F_; c0 = (cb - 92) * 64; }
  const int rs = lane >> 3, cc = lane & 7;
  u32x4 wv[4];
#pragma unroll
  for (int i = 0; i < 4; ++i) {
    const int row = kh * 256 + wave * 32 + i * 8 + rs;
    wv[i] = ld_nt(W + (size_t)row * ldw + c0 + cc * 8);
  }
  if (tid < 256) {
    const u32x4 xv4 = *reinterpret_cast<const u32x4*>(x + tid * 8);
    float* dst = &xs[0][0] + tid * 8;
#pragma unroll
    for (int k = 0; k < 4; ++k) {
      dst[2 * k] = __uint_as_float(xv4[k] << 16);
      dst[2 * k + 1] = __uint_as_float(xv4[k] & 0xffff0000u);
    }
  }
  __syncthreads();
  float acc[B_][8];
#pragma unroll
  for (int b = 0; b < B_; ++b)
#pragma unroll
    for (int j = 0; j < 8; ++j) acc[b][j] = 0.f;
#pragma unroll
  for (int i = 0; i < 4; ++i) {
    const int row = kh * 256 + wave * 32 + i * 8 + rs;
    float w[8];
#pragma unroll
    for (int k = 0; k < 4; ++k) {
      w[2 * k] = __uint_as_float(wv[i][k] << 16);
      w[2 * k + 1] = __uint_as_float(wv[i][k] & 0xffff0000u);
    }
#pragma unroll
    for (int b = 0; b < B_; ++b) {
      const float xv = xs[b][row];
#pragma unroll
      for (int j = 0; j < 8; ++j) acc[b][j] = fmaf(xv, w[j], acc[b][j]);
    }
  }
#pragma unroll
  for (int b = 0; b < B_; ++b)
#pragma unroll
    for (int j = 0; j < 8; ++j) {
      float v = acc[b][j];
      v += __shfl_xor(v, 8);
      v += __shfl_xor(v, 16);
      v += __shfl_xor(v, 32);
      acc[b][j] = v;
    }
  if (rs == 0) {
#pragma unroll
    for (int b = 0; b < B_; ++b)
#pragma unroll
      for (int j = 0; j < 8; ++j) red[wave][b][cc * 8 + j] = acc[b][j];
  }
  __syncthreads();
  if (tid < B_ * 64) {
    const int b = tid >> 6, col = tid & 63;
    float s = 0.f;
#pragma unroll
    for (int w = 0; w < 8; ++w) s += red[w][b][col];
    ws[WS_PROJ + ((size_t)kh * B_ + b) * NPROJ + cb * 64 + col] = s;
  }
}

constexpr unsigned SPIN_CAP = 1u << 20;
__global__ void __launch_bounds__(512, 2) kb_attn(const __hip_bfloat16* __restrict__ kc,
                                                  const __hip_bfloat16* __restrict__ vc,
                                                  const __hip_bfloat16* __restrict__ wo,
                                                  const __hip_bfloat16* __restrict__ wd,
                                                  const __hip_bfloat16* __restrict__ x,
                                                  const __hip_bfloat16* __restrict__ gw,
                                                  float* __restrict__ ws,
                                                  __hip_bfloat16* __restrict__ out) {
  __shared__ __attribute__((aligned(16))) float pex[8][64][4];
  __shared__ __attribute__((aligned(16))) float mrg[8][GQ][DH + 4];
  __shared__ __attribute__((aligned(16))) unsigned qs[GQ * DH / 2];
  __shared__ __attribute__((aligned(16))) float act[GQ * DH + FROWS + 32];
  __shared__ __attribute__((aligned(16))) float red[8][2][64];
  const int tid = threadIdx.x, lane = tid & 63, wave = tid >> 6;
  const int s = blockIdx.x, kvh = blockIdx.y, b = blockIdx.z;
  const int r = lane >> 4, c = lane & 15;
  constexpr float C2 = 0.088388347648318440f * 1.4426950408889634f;
  const size_t key0 = (size_t)(SK - WIN) + s * KEYS_PER_SPLIT + wave * 16;
  const __hip_bfloat16* kb = kc + ((size_t)(b * HKV + kvh) * SK + key0) * DH;
  const __hip_bfloat16* vb = vc + ((size_t)(b * HKV + kvh) * SK + key0) * DH;
  unsigned long long* gpo = reinterpret_cast<unsigned long long*>(ws + WS_GR);
  unsigned long long* gy = gpo + NGR_PO;

  STAMPAT(0);
  u32x4 kf[4][4];
  if (wave >= 2) {
#pragma unroll
    for (int j = 0; j < 4; ++j) kf[0][j] = ld_nt(kb + (size_t)c * DH + 32 * j + 8 * r);
  }
  const float* pj = ws + WS_PROJ;
  if (wave == 0) {
    f32x4 qa[2], qb[2];
#pragma unroll
    for (int i = 0; i < 2; ++i) {
      qa[i] = *reinterpret_cast<const f32x4*>(pj + (size_t)b * NPROJ + kvh * 512 + lane * 8 + 4 * i);
      qb[i] = *reinterpret_cast<const f32x4*>(pj + (size_t)(B_ + b) * NPROJ + kvh * 512 + lane * 8 + 4 * i);
    }
    const f32x4 s0 = qa[0] + qb[0], s1 = qa[1] + qb[1];
    *reinterpret_cast<u32x4*>(&qs[lane * 4]) =
        u32x4{pk_bf16(s0[0], s0[1]), pk_bf16(s0[2], s0[3]), pk_bf16(s1[0], s1[1]), pk_bf16(s1[2], s1[3])};
  } else if (wave == 1) {
    float hv[4][4];
#pragma unroll
    for (int i = 0; i < 4; ++i) {
      const int fcl = kvh * FROWS + min(lane + 64 * i, FROWS - 1);
      hv[i][0] = pj[(size_t)b * NPROJ + HQ * DH + fcl];
      hv[i][1] = pj[(size_t)(B_ + b) * NPROJ + HQ * DH + fcl];
      hv[i][2] = pj[(size_t)b * NPROJ + HQ * DH + F_ + fcl];
      hv[i][3] = pj[(size_t)(B_ + b) * NPROJ + HQ * DH + F_ + fcl];
    }
#pragma unroll
    for (int i = 0; i < 4; ++i) {
      const float gv = rbf(hv[i][0] + hv[i][1]), uv = rbf(hv[i][2] + hv[i][3]);
      act[GQ * DH + lane + 64 * i] = rbf((gv > 0.f ? gv : 0.f) * uv);
    }
  }
  if (wave < 2) {
#pragma unroll
    for (int j = 0; j < 4; ++j) kf[0][j] = ld_nt(kb + (size_t)c * DH + 32 * j + 8 * r);
  }
  const int rs = lane >> 3, cc = lane & 7;
  u32x4 wv[12];
#pragma unroll
  for (int i = 0; i < 8; ++i)
    wv[i] = *reinterpret_cast<const u32x4*>(wo + (size_t)(kvh * 512 + i * 64 + wave * 8 + rs) * D_ + s * 64 + cc * 8);
#pragma unroll
  for (int i = 0; i < 4; ++i) {
    const int row = min(i * 64 + wave * 8 + rs, FROWS - 1);
    wv[8 + i] = *reinterpret_cast<const u32x4*>(wd + (size_t)(kvh * FROWS + row) * D_ + s * 64 + cc * 8);
  }
  lds_barrier();
  STAMPAT(1);
#pragma unroll
  for (int t = 1; t < 4; ++t)
#pragma unroll
    for (int j = 0; j < 4; ++j) kf[t][j] = ld_nt(kb + (size_t)(128 * t + c) * DH + 32 * j + 8 * r);
  u32x4 vf[16];
#pragma unroll
  for (int i = 0; i < 16; ++i) vf[i] = ld_nt(vb + (size_t)(128 * (i >> 2) + 4 * (i & 3) + r) * DH + 8 * c);
  __builtin_amdgcn_sched_barrier(0);

  u32x4 qf[4];
#pragma unroll
  for (int j = 0; j < 4; ++j) qf[j] = *reinterpret_cast<const u32x4*>(&qs[((c & 3) * DH + 32 * j + 8 * r) >> 1]);

  f32x4 sacc = {0.f, 0.f, 0.f, 0.f};
#pragma unroll
  for (int t = 0; t < 4; ++t) {
    const bool mine = (c >> 2) == t;
#pragma unroll
    for (int j = 0; j < 4; ++j) {
      const u32x4 qz = mine ? qf[j] : u32x4{0u, 0u, 0u, 0u};
      sacc = __builtin_amdgcn_mfma_f32_16x16x32_bf16(__builtin_bit_cast(bf16x8, kf[t][j]),
                                                     __builtin_bit_cast(bf16x8, qz), sacc, 0, 0, 0);
    }
  }
  float sc[4];
#pragma unroll
  for (int i = 0; i < 4; ++i) sc[i] = sacc[i] * C2;
  float m = fmaxf(fmaxf(sc[0], sc[1]), fmaxf(sc[2], sc[3]));
  m = fmaxf(m, __shfl_xor(m, 4));
  m = fmaxf(m, __shfl_xor(m, 8));
  m = fmaxf(m, __shfl_xor(m, 16));
  m = fmaxf(m, __shfl_xor(m, 32));
  float l = 0.f;
#pragma unroll
  for (int i = 0; i < 4; ++i) {
    const float p = __builtin_amdgcn_exp2f(sc[i] - m);
    l += p;
    pex[wave][16 * (c >> 2) + 4 * r + i][c & 3] = p;
  }
  l += __shfl_xor(l, 4);
  l += __shfl_xor(l, 8);
  l += __shfl_xor(l, 16);
  l += __shfl_xor(l, 32);
  __builtin_amdgcn_s_waitcnt(0xc07f);
  __builtin_amdgcn_wave_barrier();
  STAMPAT(2);
  f32x2 acc[GQ][4];
#pragma unroll
  for (int g = 0; g < GQ; ++g)
#pragma unroll
    for (int k = 0; k < 4; ++k) acc[g][k] = f32x2{0.f, 0.f};
#pragma unroll
  for (int i = 0; i < 16; ++i) {
    const f32x4 pr = *reinterpret_cast<const f32x4*>(&pex[wave][4 * i + r][0]);
    f32x2 v2[4];
#pragma unroll
    for (int k = 0; k < 4; ++k)
      v2[k] = f32x2{__uint_as_float(vf[i][k] << 16), __uint_as_float(vf[i][k] & 0xffff0000u)};
#pragma unroll
    for (int g = 0; g < GQ; ++g) {
      const f32x2 pg = f32x2{pr[g], pr[g]};
#pragma unroll
      for (int k = 0; k < 4; ++k) acc[g][k] = __builtin_elementwise_fma(pg, v2[k], acc[g][k]);
    }
  }
#pragma unroll
  for (int g = 0; g < GQ; ++g)
#pragma unroll
    for (int k = 0; k < 4; ++k) {
      float a0 = acc[g][k][0], a1 = acc[g][k][1];
      a0 += __shfl_xor(a0, 16); a1 += __shfl_xor(a1, 16);
      a0 += __shfl_xor(a0, 32); a1 += __shfl_xor(a1, 32);
      acc[g][k] = f32x2{a0, a1};
    }
  if (r == 0) {
#pragma unroll
    for (int g = 0; g < GQ; ++g) {
#pragma unroll
      for (int k = 0; k < 4; k += 2)
        *reinterpret_cast<f32x4*>(&mrg[wave][g][4 + 8 * c + 2 * k]) =
            f32x4{acc[g][k][0], acc[g][k][1], acc[g][k + 1][0], acc[g][k + 1][1]};
    }
    if (c < GQ) { mrg[wave][c][0] = m; mrg[wave][c][1] = l; }
  }
  lds_barrier();
  STAMPAT(3);
  const int g = tid >> 7, d = tid & 127;
  {
    float M = -INFINITY;
#pragma unroll
    for (int w = 0; w < 8; ++w) M = fmaxf(M, mrg[w][g][0]);
    float L = 0.f, o = 0.f;
#pragma unroll
    for (int w = 0; w < 8; ++w) {
      const float wt = __builtin_amdgcn_exp2f(mrg[w][g][0] - M);
      L = fmaf(wt, mrg[w][g][1], L);
      o = fmaf(wt, mrg[w][g][4 + d], o);
    }
    unsigned long long* rec = gpo + (((size_t)b * HQ + kvh * GQ + g) * NSPLIT + s) * PREC;
    st_gr(rec + 2 + d, o);
    if (d == 0) st_gr(rec, M);
    if (d == 1) st_gr(rec + 1, L);
  }
  STAMPAT(4);
  {
    const unsigned long long* rec0 = gpo + ((size_t)b * HQ + kvh * GQ + g) * NSPLIT * PREC;
    unsigned long long gm[NSPLIT], gl[NSPLIT], ga[NSPLIT];
    for (unsigned spins = 0; spins < SPIN_CAP; ++spins) {
      bool ok = true;
#pragma unroll
      for (int sp = 0; sp < NSPLIT; ++sp) {
        gm[sp] = ld_gr(rec0 + (size_t)sp * PREC);
        gl[sp] = ld_gr(rec0 + (size_t)sp * PREC + 1);
        ga[sp] = ld_gr(rec0 + (size_t)sp * PREC + 2 + d);
      }
#pragma unroll
      for (int sp = 0; sp < NSPLIT; ++sp)
        ok = ok && ((unsigned)(gm[sp] >> 32) == TAG) && ((unsigned)(gl[sp] >> 32) == TAG) && ((unsigned)(ga[sp] >> 32) == TAG);
      if (__all(ok)) break;
      __builtin_amdgcn_s_sleep(1);
    }
    float M = -INFINITY;
#pragma unroll
    for (int sp = 0; sp < NSPLIT; ++sp) M = fmaxf(M, __uint_as_float((unsigned)gm[sp]));
    float L = 0.f, o = 0.f;
#pragma unroll
    for (int sp = 0; sp < NSPLIT; ++sp) {
      const float wt = __builtin_amdgcn_exp2f(__uint_as_float((unsigned)gm[sp]) - M);
      L = fmaf(wt, __uint_as_float((unsigned)gl[sp]), L);
      o = fmaf(wt, __uint_as_float((unsigned)ga[sp]), o);
    }
    act[tid] = rbf(o / L);
  }
  lds_barrier();
  STAMPAT(5);
  {
    float ya[8], yf[8];
#pragma unroll
    for (int j = 0; j < 8; ++j) { ya[j] = 0.f; yf[j] = 0.f; }
#pragma unroll
    for (int i = 0; i < 12; ++i) {
      const int row = (i < 8) ? (i * 64 + wave * 8 + rs) : (GQ * DH + (i - 8) * 64 + wave * 8 + rs);
      const float av = (i < 8 || (row - GQ * DH) < FROWS) ? act[row] : 0.f;
      float* y = (i < 8) ? ya : yf;
#pragma unroll
      for (int k = 0; k < 4; ++k) {
        y[2 * k] = fmaf(av, __uint_as_float(wv[i][k] << 16), y[2 * k]);
        y[2 * k + 1] = fmaf(av, __uint_as_float(wv[i][k] & 0xffff0000u), y[2 * k + 1]);
      }
    }
#pragma unroll
    for (int j = 0; j < 8; ++j) {
      float v = ya[j], w = yf[j];
      v += __shfl_xor(v, 8); w += __shfl_xor(w, 8);
      v += __shfl_xor(v, 16); w += __shfl_xor(w, 16);
      v += __shfl_xor(v, 32); w += __shfl_xor(w, 32);
      ya[j] = v; yf[j] = w;
    }
    if (rs == 0) {
#pragma unroll
      for (int j = 0; j < 8; ++j) { red[wave][0][cc * 8 + j] = ya[j]; red[wave][1][cc * 8 + j] = yf[j]; }
    }
  }
  lds_barrier();
  STAMPAT(6);
  unsigned long long* yb = gy + (size_t)(b * NSPLIT + s) * HKV * 128;
  if (tid < 128) {
    float s0 = 0.f;
#pragma unroll
    for (int w = 0; w < 8; ++w) s0 += red[w][tid >> 6][tid & 63];
    st_gr(yb + kvh * 128 + tid, s0);
  }
  STAMPAT(7);
  if (kvh == 0 && tid < 64) {
    const u32x4 xr = *reinterpret_cast<const u32x4*>(x + b * D_ + lane * 8);
    const u32x4 gr = *reinterpret_cast<const u32x4*>(gw + lane * 8);
    unsigned long long ga[HKV], gf[HKV];
    for (unsigned spins = 0; spins < SPIN_CAP; ++spins) {
      bool ok = true;
#pragma unroll
      for (int h = 0; h < HKV; ++h) { ga[h] = ld_gr(yb + h * 128 + lane); gf[h] = ld_gr(yb + h * 128 + 64 + lane); }
#pragma unroll
      for (int h = 0; h < HKV; ++h) ok = ok && ((unsigned)(ga[h] >> 32) == TAG) && ((unsigned)(gf[h] >> 32) == TAG);
      if (__all(ok)) break;
      __builtin_amdgcn_s_sleep(1);
    }
    float dd = 0.f;
#pragma unroll
    for (int k = 0; k < 4; ++k) {
      dd += rbf(__uint_as_float(xr[k] << 16) * __uint_as_float(gr[k] << 16));
      dd += rbf(__uint_as_float(xr[k] & 0xffff0000u) * __uint_as_float(gr[k] & 0xffff0000u));
    }
#pragma unroll
    for (int o = 1; o < 64; o <<= 1) dd += __shfl_xor(dd, o);
    const float gt = rbf(1.f / (1.f + __expf(-rbf(dd))));
    float a = 0.f, f = 0.f;
#pragma unroll
    for (int h = 0; h < HKV; ++h) { a += __uint_as_float((unsigned)ga[h]); f += __uint_as_float((unsigned)gf[h]); }
    a = rbf(a);
    f = rbf(f);
    const float res = rbf(gt * a) + rbf(rbf(1.f - gt) * f);
    out[b * D_ + s * 64 + lane] = __float2bfloat16(res);
  }
  STAMPAT(8);
}

}

extern "C" void kernel_launch(void* const* d_in, const int* in_sizes, int n_in,
                              void* d_out, int out_size, void* d_ws, size_t ws_size,
                              hipStream_t stream) {
  const __hip_bfloat16* x = (const __hip_bfloat16*)d_in[0];
  const __hip_bfloat16* kc = (const __hip_bfloat16*)d_in[1];
  const __hip_bfloat16* vc = (const __hip_bfloat16*)d_in[2];
  const __hip_bfloat16* wq = (const __hip_bfloat16*)d_in[3];
  const __hip_bfloat16* wo = (const __hip_bfloat16*)d_in[4];
  const __hip_bfloat16* wg = (const __hip_bfloat16*)d_in[6];
  const __hip_bfloat16* wu = (const __hip_bfloat16*)d_in[7];
  const __hip_bfloat16* wd = (const __hip_bfloat16*)d_in[8];
  const __hip_bfloat16* gw = (const __hip_bfloat16*)d_in[9];
  float* ws = (float*)d_ws;
  ka_proj<<<dim3(2 * NPROJ / 64), dim3(512), 0, stream>>>(x, wq, wg, wu, ws);
  kb_attn<<<dim3(NSPLIT, HKV, B_), dim3(512), 0, stream>>>(kc, vc, wo, wd, x, gw, ws,
                                                           (__hip_bfloat16*)d_out);
}
```
